```python
import torch

def _softmax(x, axis=-1):
    return torch.softmax(x, dim=axis)
def _gelu(x):
    return torch.nn.functional.gelu(x, approximate="tanh")
def _silu(x):
    return torch.nn.functional.silu(x)
def _sigmoid(x):
    return torch.sigmoid(x)
def _layernorm(x, axis=-1, eps=1e-5):
    m = x.mean(dim=axis, keepdim=True)
    v = x.var(dim=axis, keepdim=True, unbiased=False)
    return (x - m) / torch.sqrt(v + eps)
def _scatter_add(tgt, idx, src, axis=0):
    return tgt.clone().index_add_(axis, idx, src)
def _segment_sum(src, seg, n):
    out = torch.zeros((n,) + src.shape[1:], dtype=src.dtype)
    return out.index_add_(0, seg, src)
def _pad(x, pw):
    flat = tuple(v for pair in reversed(pw) for v in pair)
    return torch.nn.functional.pad(x, flat)

kernel_name = 'gated_mix__softmax_full_mha__swiglu_ffn__prompt'

def setup_inputs():

    _w = (lambda a: (lambda t: t.to(torch.bfloat16) if t.is_floating_point() else t)(torch.as_tensor(a)))
    def _A_setup():
        B, S, D = 1, 2048, 512
        return {
            "x": _w((lambda *s: torch.randn(*s, dtype=torch.bfloat16) * 0.05)(B, S, D)),
            "wqkv": _w((lambda *s: torch.randn(*s, dtype=torch.bfloat16) * 0.8)(D, 3 * D)),
            "wo": _w((lambda *s: torch.randn(*s, dtype=torch.bfloat16) * 0.05)(D, D)),
        }
    _A_in = _A_setup()

    def _B_setup():
        B, S, D, F = 1, 2048, 512, 1376
        return {
            "x": _w((lambda *s: torch.randn(*s, dtype=torch.bfloat16) * 0.05)(B, S, D)),
            "w_gate": _w((lambda *s: torch.randn(*s, dtype=torch.bfloat16) * 0.1)(D, F)),
            "w_up": _w((lambda *s: torch.randn(*s, dtype=torch.bfloat16) * 0.1)(D, F)),
            "w_down": _w((lambda *s: torch.randn(*s, dtype=torch.bfloat16) * 0.1)(F, D)),
        }
    _B_in = _B_setup()

    return {
        "A_x": _A_in["x"],
        "A_wqkv": _A_in["wqkv"],
        "A_wo": _A_in["wo"],
        "B_x": _B_in["x"],
        "B_w_gate": _B_in["w_gate"],
        "B_w_up": _B_in["w_up"],
        "B_w_down": _B_in["w_down"],
        "gate_w": _w((lambda *s: torch.randn(*s, dtype=torch.bfloat16) * 0.05)(512) * 16.0),
    }

def reference(A_x, A_wqkv, A_wo, B_x, B_w_gate, B_w_up, B_w_down, gate_w):
    def _A_ref(x, wqkv, wo):
        B, S, D = x.shape
        H = 32; Dh = D // H
        qkv = (lambda x, *d: x.permute(*d))((x @ wqkv).reshape(B, S, 3, H, Dh), 2, 0, 3, 1, 4)
        q, k, v = qkv[0], qkv[1], qkv[2]
        scale = Dh ** -0.5
        scores = torch.einsum("bhqd,bhkd->bhqk", q, k) * scale
        p = _softmax(scores, axis=-1)
        o = torch.einsum("bhqk,bhkd->bhqd", p, v)
        return (lambda x, *d: x.permute(*d))(o, 0, 2, 1, 3).reshape(B, S, D) @ wo

    def _B_ref(x, w_gate, w_up, w_down):
        g = _silu(x @ w_gate)
        u = x @ w_up
        return (g * u) @ w_down

    x = A_x
    g = _sigmoid((x * gate_w).sum(-1, keepdims=True))
    a = _A_ref(x, A_wqkv, A_wo)
    b = _B_ref(x, B_w_gate, B_w_up, B_w_down)
    return g * a + (1 - g) * b
```

```cpp
#include <hip/hip_runtime.h>
#include <hip/hip_bf16.h>
#include <stdint.h>
#include <type_traits>

typedef __attribute__((ext_vector_type(8))) __bf16 bf16x8;
typedef __attribute__((ext_vector_type(2))) __bf16 bf16x2;
typedef __attribute__((ext_vector_type(2))) float f32x2;
typedef __attribute__((ext_vector_type(4))) float f32x4;
typedef __attribute__((ext_vector_type(16))) float f32x16;
typedef __attribute__((ext_vector_type(4))) unsigned u32x4;
typedef __attribute__((ext_vector_type(2))) unsigned u32x2;
typedef short v4i16_t __attribute__((ext_vector_type(4)));
typedef unsigned long long u64;
typedef __attribute__((ext_vector_type(8))) unsigned long long u64x8;
#define LAS __attribute__((address_space(3)))
#define GAS __attribute__((address_space(1)))
#define CAS __attribute__((address_space(4)))
typedef __amdgpu_buffer_rsrc_t rsrc_t;

#define SBAR() __builtin_amdgcn_sched_barrier(0)
template <class T> __device__ __forceinline__ void PIN(T& x) { asm volatile("" : "+v"(x)); }
template <class T> __device__ __forceinline__ void SPIN_(T& x) { asm volatile("" : "+s"(x)); }
#define RLX __ATOMIC_RELAXED, __HIP_MEMORY_SCOPE_AGENT

constexpr int SEQ = 2048, DM = 512, FF = 1376;
constexpr int WS_F1 = 4096, WS_F2 = 8192, WS_G = 16384, WS_DBG = 32768;
constexpr int WS_Q = 1 << 20, WS_K = WS_Q + (2 << 20), WS_V = WS_K + (2 << 20), WS_O = WS_V + (2 << 20), WS_H = WS_O + (2 << 20);
constexpr int WS_END = WS_H + 64 * 86 * 1024;
constexpr int SLOT1 = 28672, L_GW = 5 * SLOT1, UPOFF = 12288 + 32;
constexpr int L_KV = 98304, L_CONST = 147456, L_RED = 65536, L_CNT = 155648, L_XL = L_CNT + 512, LDS_BYTES = 163840;
constexpr float LOG2E = 1.4426950408889634f;
constexpr int SPIN_CAP = 1 << 18;

template <int I, int N, class F> __device__ __forceinline__ void sfor(F&& f) { if constexpr (I < N) { f(std::integral_constant<int, I>{}); sfor<I + 1, N>(f); } }
__device__ __forceinline__ f32x16 mfma(u32x4 a, u32x4 b, f32x16 c) { return __builtin_amdgcn_mfma_f32_32x32x16_bf16(__builtin_bit_cast(bf16x8, a), __builtin_bit_cast(bf16x8, b), c, 0, 0, 0); }
__device__ __forceinline__ unsigned pk(float lo, float hi) { f32x2 v = {lo, hi}; return __builtin_bit_cast(unsigned, __builtin_convertvector(v, bf16x2)); }
typedef __attribute__((ext_vector_type(16))) __bf16 bf16x16;
typedef __attribute__((ext_vector_type(8))) float f32x8;
template <int O> __device__ __forceinline__ f32x16 mfmah(u32x4 a, const f32x16& s, f32x16 c, unsigned sel) {
    const f32x8 b = __builtin_shufflevector(s, s, O, O + 1, O + 2, O + 3, O + 4, O + 5, O + 6, O + 7);
    return __builtin_amdgcn_smfmac_f32_32x32x32_bf16(__builtin_bit_cast(bf16x8, a), __builtin_bit_cast(bf16x16, b), c, sel, 0, 0);
}
#define PERM 0
#define NOM 0
#define CK 2
#define HP 14
#define HM 0
#define SPM 1
__device__ __forceinline__ unsigned pt(float lo, float hi) {
    return pk(lo, hi);
}
template <int N> __device__ __forceinline__ void waitbar() { asm volatile("s_waitcnt vmcnt(%0) lgkmcnt(0)\n\ts_barrier" ::"n"(N) : "memory"); }
template <int IMM> __device__ __forceinline__ void dma(rsrc_t rs, unsigned voff, unsigned soff, unsigned ldsa) {
    asm volatile("s_add_u32 m0, %3, %4\n\ts_nop 0\n\tbuffer_load_dwordx4 %0, %1, %2 offen lds" ::"v"(voff), "s"(rs), "s"(soff), "s"(ldsa), "n"(IMM) : "memory", "scc");
}
__device__ __forceinline__ void touch(rsrc_t rs, unsigned voff, unsigned ldsa) {
    asm volatile("s_mov_b32 m0, %2\n\ts_nop 0\n\tbuffer_load_dword %0, %1, 0 offen lds" ::"v"(voff), "s"(rs), "s"(ldsa) : "memory");
}
template <int N> __device__ __forceinline__ void vmbar() { asm volatile("s_waitcnt vmcnt(%0)\n\ts_barrier" ::"n"(N) : "memory"); }
__device__ __forceinline__ u32x4 lds128(unsigned a) { return *(const LAS u32x4*)(uintptr_t)a; }
__device__ __forceinline__ void lds128w(unsigned a, u32x4 v) { *(LAS u32x4*)(uintptr_t)a = v; }
__device__ __forceinline__ u32x2 ldstr(unsigned a) { return __builtin_bit_cast(u32x2, __builtin_amdgcn_ds_read_tr16_b64_v4i16((LAS v4i16_t*)(uintptr_t)a)); }
__device__ __forceinline__ u32x4 ldstr2(unsigned a, int d) { u32x2 lo = ldstr(a), hi = ldstr(a + d); return (u32x4){lo[0], lo[1], hi[0], hi[1]}; }
__device__ __forceinline__ u32x4 ldc(rsrc_t rs, unsigned voff, unsigned soff) { return __builtin_amdgcn_raw_buffer_load_b128(rs, voff, soff, 16); }
__device__ __forceinline__ void stc(u32x4 v, rsrc_t rs, unsigned voff, unsigned soff) { __builtin_amdgcn_raw_buffer_store_b128(v, rs, voff + soff, 0, 16); }
typedef __attribute__((ext_vector_type(32))) unsigned u32x32;
#define STR_(x) #x
#define STR(x) STR_(x)
__device__ __forceinline__ void rts(u64& r) { asm volatile("s_memrealtime %0" : "=s"(r)::"memory"); }
__device__ __forceinline__ void poll(const char* p, u64 tag) {
    #pragma unroll 1
    for (int it = 0; it < SPIN_CAP; ++it) {
        u64 v = __hip_atomic_load((const GAS u64*)p, RLX);
        if (__all(v == tag)) break;
        __builtin_amdgcn_s_sleep(1);
    }
    __builtin_amdgcn_fence(__ATOMIC_ACQUIRE, "wavefront");
}
__device__ __forceinline__ u32x4 pack8(const f32x16& a, int o, float s) {
    return (u32x4){pk(a[o] * s, a[o + 1] * s), pk(a[o + 2] * s, a[o + 3] * s), pk(a[o + 4] * s, a[o + 5] * s), pk(a[o + 6] * s, a[o + 7] * s)};
}
struct TS { u64 r[10], c[10], t, e[4]; unsigned v; };
__device__ __forceinline__ void ts_(u64& r, u64& c) { asm volatile("s_memrealtime %0\n\ts_memtime %1" : "=s"(r), "=s"(c)); }
#define STAMP(i)
#define SEQ_ORDER 0, 1, 2, 3, 4, 5, 6, 7, 8, 9, 10, 11, 12, 13, 14, 15
#define PUB_AT 42
#define ROT (j + (j >> 3) + 4 * (c8 & 1))
#define POLL1 12
#define B21 (POLL1 + 10)
#define POLL2 20
#define B22 22
#define KW2 99
#define STAG 1
#define STQ 1
#define PBAR 0
#define NQ 2
#define PRA 9
#define PRB 13
#define PRC 15
#define DIRECT 2
#define PUBH 4
#define PIPE 0
#define OLAY 1
#define LAQ 12
#define LAN 8
#define SPREAD 1
#define T4 8
#define T3 8
#define E3 4
#define T2 4
#define E4 0
#define GF 0
#define EO 0
#define X4 4
#define EARLY 0
#define DELAY 0
#define RM 0
#define LD 4
#define S5E 0
#define ROTC 1
#define PM1 24
#define PG 50
#define THR 4
#define SL 2
#define OF_AT 0
#define B2_AT (POLL_AT + 10)
#define KW_AT 33
#define WSEL 0
#define POLL_AT 12
#define MT "s_memtime %0"
template <int PREV> __device__ __forceinline__ void stg_(TS& s) { SBAR(); SPIN_(s.t); asm volatile("v_writelane_b32 %0, %1, %2" : "+v"(s.v) : "s"((unsigned)s.t), "n"(PREV)); asm volatile(MT : "=s"(s.t)); SBAR(); }
#define STE(i)
#define STEW()
#define STG(i)

struct P1 {
    rsrc_t rsA, rsB, rsW;
    unsigned voA[4], voB[3];
    unsigned soA, soB;
    unsigned dA, dB;
    unsigned rot;
    unsigned xa[4], wa, ga, gwa;
    unsigned sto, hso, kvo, kzo, kzk, oo, qo[NQ];
    unsigned kw;
    float qs0, qs1;
    char *ws, *f1, *fp;
    unsigned lane; bool gst;
};
constexpr int POS[16] = {SEQ_ORDER};
constexpr int lastq() { int l = 0; for (int i = 0; i < 16; ++i) if (POS[i] < 8) l = i; return l; }
constexpr int PQ = lastq();
constexpr unsigned slotof(int pos) { return ((pos + 2) % 5) * SLOT1; }
template <int P> constexpr int npc(int pos) { return pos > 15 ? 0 : (POS[pos] < 8 ? (P == 0 ? 3 : 4) : 3); }
struct Sched { int cnt[56], op[56][12]; int last[16]; };
template <int P> constexpr Sched mk_sched() {
    Sched r{}; int n = 0;
    for (int s = 0; s < 16; ++s) for (int i = 0; i < npc<P>(s); ++i) {
        int e = 4 * (s - 3), at = 0; if (s <= LAN && e < 4 * s - LAQ) e = 4 * s - LAQ;
        if (e + i < 0) at = -1; else if (s == 15) at = 44 + i; else { if (n < e) n = e; at = n++; }
        r.op[at + 1][r.cnt[at + 1]++] = 4 * s + i; r.last[s] = at;
    }
    return r;
}
template <int P> constexpr Sched SCH = mk_sched<P>();
template <int P> constexpr int pend(int g) {
    const int need = (g + 2) / 4; int cnt = 0;
    auto op = [&](int tag) { if (tag == need) cnt = 0; else ++cnt; };
    for (int s = -3; s < g; ++s) {
        const bool out_first = !(STQ || (STAG && P == 0));
        if (out_first && s == 4 * PQ + 4) for (int i = 0; i < (P == 0 ? 4 : 2); ++i) op(-1);
        for (int i = 0; i < SCH<P>.cnt[s + 3]; ++i) op(SCH<P>.op[s + 3][i] / 4);
        if (!out_first && s == 4 * PQ + 4) for (int i = 0; i < (P == 0 ? 4 : 2); ++i) op(g >= PUB_AT ? need : -1);
    }
    return cnt;
}
template <int P, int S, int I> __device__ __forceinline__ void p1_dma(const P1& c) {
    constexpr int slot = slotof(S);
    if constexpr (I < npc<P>(S)) {
        const unsigned ch = (POS[S] + c.rot) & 7;
        if constexpr (POS[S] < 8) dma<slot + I * 1024>(c.rsA, c.voA[I], c.soA + ch * (P == 0 ? 64 * 3072 : 128), c.dA);
        else dma<slot + I * 1024>(c.rsB, c.voB[I], c.soB + ch * (64 * 2752), c.dB);
    }
}
template <int P, int N> __device__ __forceinline__ void p1_req(const P1& c) {
    sfor<0, SCH<P>.cnt[N + 1]>([&](auto i) { constexpr int o = SCH<P>.op[N + 1][i]; if constexpr (o >= 4) p1_dma<P, o / 4, o % 4>(c); });
}
template <int P> constexpr bool sched_ok() { for (int s = 0; s < 16; ++s) if (SCH<P>.last[s] >= (s < 13 ? 4 * s : 52)) return false; return true; }
static_assert(sched_ok<0>() && sched_ok<1>(), "");
struct KV0 { u32x4 q[NQ], kf; f32x16 negm, s; unsigned ka; };

template <int P, bool ISV, int MODE, class F>
__device__ __forceinline__ void p1_loop(const P1& c, u64& epoch, u64& tag, KV0& kv, F&& setup, TS& pt1) {
    constexpr int NT = P == 0 ? 2 : 1, NG = MODE == 2 ? 2 : 3, NM = 12 * NG;
    constexpr bool FOUR = MODE == 1;
    constexpr int PA = MODE == 2 ? POLL2 : P ? POLL1 : POLL_AT, B2 = MODE == 2 ? B22 : P ? B21 : B2_AT, KW = MODE == 2 ? KW2 : KW_AT;
    constexpr int SP = MODE == 2 ? 0 : SPREAD;
    f32x16 aq[NT], ag[NG]; u32x4 wf[3][3], xf[32];
    char* ws = c.ws; const unsigned lane = c.lane;
    sfor<0, NT>([&](auto t) { aq[t] = f32x16{}; });
    sfor<0, NG>([&](auto t) { ag[t] = f32x16{}; });
    SBAR();
    p1_req<P, -1>(c);
    STE(3);
    waitbar<pend<P>(-2)>();
    STG(1); STEW();
    unsigned wc;
    u32x4 wt[4]; unsigned tb[3];
    auto rdt = [&](auto mm) {
        constexpr int m = mm, t = FOUR ? (m / 12 + 2) % 3 : m / 12, kk = m % 12;
        if constexpr (m < NM) {
            if constexpr (FOUR && t == 2) wt[m & 3] = lds128(c.gwa + (20 + kk) * 32);
            else wt[m & 3] = ldstr2(tb[kk / 4] + (kk % 4) * 3072 + t * 32, 768);
        }
    };
    auto rd = [&](auto nn) {
        constexpr int n = nn, p1 = n / 4, k1 = n % 4, ch = POS[p1] & 7;
        if constexpr (n == 52) {
            sfor<0, 3>([&](auto i) { tb[i] = c.ga + slotof(13 + i); PIN(tb[i]); });
            sfor<0, 3>([&](auto i) { rdt(i); });
        } else if constexpr (n > 52) {
        } else if constexpr (POS[p1] < 8) {
            if constexpr (k1 == 0) { wc = c.wa + slotof(p1); PIN(wc); }
            unsigned xc = c.xa[k1] + slotof(p1); if constexpr (slotof(p1) > 65535) PIN(xc);
            xf[4 * ch + k1] = lds128(xc);
            sfor<0, NT>([&](auto t) { wf[n % 3][t] = ldstr2(wc + k1 * 3072 + t * 64, 768); });
        } else {
            if constexpr (k1 == 0) { wc = c.ga + slotof(p1); PIN(wc); }
            sfor<0, NG>([&](auto t) {
                if constexpr (FOUR && t == 2) wf[n % 3][t] = lds128(c.gwa + (4 * ch + k1) * 32);
                else wf[n % 3][t] = ldstr2(wc + k1 * 3072 + t * 32, 768);
            });
        }
    };
    auto qst = [&](auto tt, auto hh) {
        constexpr int t = tt, hf = hh;
        if constexpr (ISV) stc(pack8(aq[t], 8 * hf, 1.f), c.rsW, ((lane >> 4) & 1) * 65536 + (lane >> 5) * 256 + (lane & 15) * 16 + 512 * hf, c.sto + t * 131072);
        else stc(pack8(aq[t], 8 * hf, t == 0 ? c.qs0 : c.qs1), c.rsW, lane * 16, c.sto + t * 131072 + 65536 * hf);
    };
    rd(std::integral_constant<int, 0>{});
    rd(std::integral_constant<int, 1>{});
    SBAR();
    p1_req<P, 0>(c); p1_req<P, 1>(c);
    sfor<0, 52>([&](auto gg) {
        constexpr int g = gg, p = g / 4, ks = g % 4, n = g + 2, xi = 4 * (POS[p] & 7) + ks;
        auto mm = [&](auto t) {
            if constexpr (POS[p] >= 8) { if constexpr (t < NG) ag[t] = mfma(wf[g % 3][t], xf[xi], ag[t]); }
            else if constexpr (t < NT) aq[t] = ISV ? mfma(xf[xi], wf[g % 3][t], aq[t]) : mfma(wf[g % 3][t], xf[xi], aq[t]);
        };
        SBAR();
        mm(std::integral_constant<int, 0>{});
        SBAR();
        if constexpr (ks == 2) {
            if constexpr (g == 42) waitbar<pend<P>(g)>(); else vmbar<(g == 50 ? 0 : pend<P>(g))>();
            STG(2 + p);
            if constexpr (P == 1 && g == PG) __builtin_amdgcn_s_setprio(1);
            if constexpr (g == 2) {
                asm volatile("" : "+s"(epoch));
                u64 z = epoch + 0x9E3779B97F4A7C15ull; z = (z ^ (z >> 30)) * 0xBF58476D1CE4E5B9ull; z = (z ^ (z >> 27)) * 0x94D049BB133111EBull; tag = z ^ (z >> 31);
            }
            if constexpr (g == PUB_AT) { if (threadIdx.x == 0) __hip_atomic_store((GAS u64*)c.f1, tag, RLX); }
        }
        rd(std::integral_constant<int, n>{});
        SBAR();
        auto dm = [&]() {
            p1_req<P, n>(c);
            SBAR();
        };
        constexpr bool early = (STAG && P == 0 && POS[p] >= 8) || (STQ && g == 4 * PQ + 4);
        if constexpr (early) dm();
        if constexpr (g == 4 * PQ + 4) { sfor<0, 2>([&](auto hf) { qst(std::integral_constant<int, 0>{}, hf); }); SBAR(); }
        mm(std::integral_constant<int, 1>{});
        SBAR();
        if constexpr (NT == 2 && g == 4 * PQ + 4) { sfor<0, 2>([&](auto hf) { qst(std::integral_constant<int, NT - 1>{}, hf); }); SBAR(); }
        mm(std::integral_constant<int, 2>{});
        SBAR();
        if constexpr (g == 4 * PQ + 4) STAMP(1);
        if constexpr (!early) dm();
    });
    float he[8], hv[8]; u64 fv; u32x4 kz, k01[4]; f32x16 s0[NQ];
    auto hst = [&](auto t) { stc((u32x4){pk(hv[0], hv[1]), pk(hv[2], hv[3]), pk(hv[4], hv[5]), pk(hv[6], hv[7])}, c.rsW, lane * 16, c.hso + t * 1024); };
    auto epi = [&](auto tt, auto ii) {
        constexpr int t = tt, i = ii;
        if constexpr (i >= 2 && i < 10) { hv[i - 2] *= he[i - 2]; PIN(hv[i - 2]); }
        if constexpr (i >= 1 && i < 9) { he[i - 1] = __builtin_amdgcn_rcpf(1.f + he[i - 1]); hv[i - 1] = ag[t][i - 1] * ag[t][7 + i]; PIN(he[i - 1]); PIN(hv[i - 1]); }
        if constexpr (i < 8) { he[i] = __builtin_amdgcn_exp2f(-LOG2E * ag[t][i]); PIN(he[i]); }
        if constexpr (i == 10) hst(tt);
    };
    auto kw = [&](auto ii) {
        SBAR();
        s0[ii] = mfma(kz, kv.q[ii], f32x16{});
        lds128w(c.kw + ii * 8192, k01[ii]);
    };
    auto rowmax = [&]() {
        float mx[2];
        sfor<0, 2>([&](auto q) { mx[q] = fmaxf(s0[q][0], s0[q][1]); sfor<1, 8>([&](auto i) { mx[q] = fmaxf(fmaxf(mx[q], s0[q][2 * i]), s0[q][2 * i + 1]); }); });
        mx[0] = fmaxf(mx[0], mx[1]);
        auto rr = __builtin_amdgcn_permlane32_swap(__float_as_uint(mx[0]), __float_as_uint(mx[0]), false, false);
        mx[0] = -fmaxf(__uint_as_float(rr[0]), __uint_as_float(rr[1]));
        sfor<0, 16>([&](auto i) { kv.negm[+i] = mx[0]; });
        PIN(kv.negm);
        if constexpr (P == 1) { kv.kf = kz; kv.s = mfma(kz, kv.q[0], kv.negm); }
        SBAR();
    };
    sfor<0, NM>([&](auto mm) {
        constexpr int m = mm, t = FOUR ? (m / 12 + 2) % 3 : m / 12, kk = m % 12, e = (m - 14) / 12, te = FOUR ? (e + 2) % 3 : e;
        SBAR();
        if constexpr (P == 1 && m == PM1) { __builtin_amdgcn_s_setprio(0); SBAR(); }
        ag[t] = mfma(wt[m & 3], xf[20 + kk], ag[t]);
        SBAR();
        rdt(std::integral_constant<int, m + 3>{});
        if constexpr (m >= 14 && !(FOUR && te == 2)) epi(std::integral_constant<int, te>{}, std::integral_constant<int, (m - 14) % 12>{});
        if constexpr (m == 1) setup();
        if constexpr (FOUR && m == 14) {
            if (c.gst) {
                const float g = GF ? __builtin_amdgcn_exp2f(fminf(LOG2E * ag[2][0], 60.f)) : __builtin_amdgcn_rcpf(1.f + __builtin_amdgcn_exp2f(-LOG2E * ag[2][0]));
                if (lane < 32) __builtin_amdgcn_raw_buffer_store_b32(__builtin_bit_cast(unsigned, g), c.rsW, lane * 4 + WS_G + ((c.sto >> 10) & 63) * 128, 0, 16);
            }
        }
        if constexpr (m == PA - 8) fv = __hip_atomic_load((const GAS u64*)c.fp, RLX);
        if constexpr (m == PA) {
            STAMP(3); STG(15);
            if (!__all(fv == tag)) poll(c.fp, tag);
            __builtin_amdgcn_fence(__ATOMIC_ACQUIRE, "wavefront");
            STAMP(4); STG(16);
            kz = ldc(c.rsW, kv.ka, c.kzk);
            if constexpr (!SP) {
            sfor<0, NQ>([&](auto i) { kv.q[i] = ldc(c.rsW, lane * 16, c.qo[i]); });
            sfor<0, 2>([&](auto i) { k01[i] = ldc(c.rsW, lane * 16, c.kvo + i * (2 << 20)); });
            }
            STG(17);
        }
        if constexpr (SP && m > PA && (m - PA) % SP == 0 && (m - PA) / SP <= 4) {
            constexpr int z = (m - PA) / SP - 1;
            if constexpr (z < 2) kv.q[z] = ldc(c.rsW, lane * 16, c.qo[z]); else k01[z - 2] = ldc(c.rsW, lane * 16, c.kvo + (z - 2) * (2 << 20));
        }
        if constexpr (m == B2 || m == B2 + 1) k01[2 + m - B2] = ldc(c.rsW, lane * 16, c.kvo + 8192 / NQ + (m - B2) * (2 << 20));
        if constexpr (m == (B2 + 2 < NM ? B2 + 2 : PA - 2)) stc((u32x4){~0u, ~0u, ~0u, ~0u}, c.rsW, lane * 16, c.oo);
        if constexpr (m == B2 + 1) STG(18);
        if constexpr (m == NM - 1) STG(19);
        if constexpr (m >= KW && m < KW + 2) kw(std::integral_constant<int, m - KW>{});
    });
    SBAR();
    STAMP(2); STG(20);
    constexpr int tl = MODE ? 1 : 2;
    hst(std::integral_constant<int, tl - 1>{});
    if constexpr (KW < NM) rowmax();
    sfor<0, 8>([&](auto i) { const float g = ag[tl][+i]; hv[i] = g * ag[tl][8 + i] * __builtin_amdgcn_rcpf(1.f + __builtin_amdgcn_exp2f(-LOG2E * g)); });
    hst(std::integral_constant<int, tl>{});
    STG(21);
    if constexpr (KW >= NM) sfor<0, 2>([&](auto i) { kw(i); });
    lds128w(c.kw + 16384, k01[2]); lds128w(c.kw + 24576, k01[3]);
    if constexpr (KW >= NM) rowmax();
}

__global__ void __launch_bounds__(512, 2)
fused_kernel(const char* __restrict__ x_, const char* __restrict__ wqkv_, const char* __restrict__ wo_, const char* __restrict__ wg_, const char* __restrict__ wu_,
             const char* __restrict__ wd_, const char* __restrict__ gw_, char* __restrict__ out_, char* ws_) {
    u64x8 ka; char* ws; TS pt1;
    STAMP(0);
    asm volatile("s_load_dwordx16 %0, %2, 0x0\n\ts_load_dwordx2 %1, %2, 0x40" : "=&s"(ka), "=&s"(ws) : "s"(__builtin_amdgcn_kernarg_segment_ptr()));
    constexpr unsigned L0 = 0;
    const unsigned tid = threadIdx.x, lane = tid & 63, r = lane & 31, h = lane >> 5;
    const int b = blockIdx.x, c8 = b & 7, j = b >> 3;
    {
        u64 pc; unsigned sz; asm volatile("s_getpc_b64 %0\n.Lk_begin:\n\ts_mov_b32 %1, .Lk_end-.Lk_begin" : "=s"(pc), "=s"(sz));
        touch(__builtin_amdgcn_make_buffer_rsrc((void*)pc, 0, sz, 0x20000), (tid & 3) == ((j >> 1) & 3) ? (tid + 512 * (j & 1)) * 128 : 0x40000000u, L0 + L_GW + 1024);
        SBAR();
    }
    const int w = __builtin_amdgcn_readfirstlane(tid >> 6);
    const unsigned cs = (lane >> 4) & 1, q4 = (lane & 15) >> 2, p4 = lane & 3;
    const int head = 4 * c8 + (j >> 3), qb = j & 7;
    const int rb = 8 * ((c8 >> 1) & 1) + (j & 7), cg = 8 * (~c8 & 1) + (c8 & 4) + (j >> 3);
    u64 tag, epoch; KV0 kv;
    constexpr int GQ = 8 / NQ;
    const int sp = w / GQ, qg = w % GQ, qt = qb * 8 + qg * NQ + sp;
    const int a = w & 3, p = w >> 2;
    P1 c;
    unsigned kr[3];
    {
        unsigned xo = (lane >> 3) * 1024 + (((lane & 7) ^ (lane >> 4)) << 4); PIN(xo);
        sfor<0, 3>([&](auto i) {
            kr[i] = ((64 * i + lane) * 171) >> 11;
            unsigned wo_ = kr[i] * 2880 + (64 * i + lane) * 16, xo_ = (xo ^ (64 * (i & 1))) + 8192 * i; PIN(wo_); PIN(xo_);
            c.voA[i] = tid < 256 ? wo_ : xo_;
        });
        c.voA[3] = (xo ^ 64) + 8192 * 3;
    }
    const int pm = -p;
    c.soA = ((a * 16 * 3072 + cg * 192) & ~pm) | (((rb * 128 + a * 32) * 1024) & pm);
    c.dA = L0 + (((16384 + a * 3072) & ~pm) | ((a * 4096) & pm));
    c.rot = ROT;
    const unsigned sstep = (64 * 3072 & ~pm) | (128 & pm);
    STE(0);
    asm volatile("s_waitcnt lgkmcnt(0)" : "+s"(ka), "+s"(ws));
    STE(1);
    const char *x = (const char*)ka[0], *wqkv = (const char*)ka[1], *wo = (const char*)ka[2], *wg = (const char*)ka[3], *wu = (const char*)ka[4], *wd = (const char*)ka[5], *gw = (const char*)ka[6];
    char* out = (char*)ka[7];
    c.rsA = __builtin_amdgcn_make_buffer_rsrc((void*)(p == 0 ? wqkv : x), 0, (DM * 3 * DM * 2 & ~pm) | (SEQ * DM * 2 & pm), 0x20000);
    sfor<0, 1 + EARLY>([&](auto s) {
        const unsigned so = c.soA + ((POS[s] + c.rot) & 7) * sstep;
        sfor<0, 3>([&](auto i) { dma<slotof(s) + i * 1024>(c.rsA, c.voA[i], so, c.dA); });
        if (p) dma<slotof(s) + 3072>(c.rsA, c.voA[3], so, c.dA);
    });
    epoch = *(const CAS u64*)ws;
    if (w == 3) dma<L_GW>(__builtin_amdgcn_make_buffer_rsrc((void*)gw, 0, DM * 2, 0x20000), (lane * 16 + c.rot * 128) & 1023, 0, L0);
    STE(2);
    SBAR();
    const rsrc_t rsW = __builtin_amdgcn_make_buffer_rsrc(ws, 0, WS_END, 0x20000);
    const int a2 = w & 1, kq = w >> 1, rt = 8 * c8 + 2 * (j >> 3) + a2, cb = j & 7;
    unsigned wda[2], wdb[2], va[3], wvo, fo;
    auto setup = [&]() {
        unsigned ln = lane; PIN(ln);
        const unsigned r = ln & 31, h = ln >> 5, cs = (ln >> 4) & 1, q4 = (ln & 15) >> 2, p4 = ln & 3;
        sfor<0, 2>([&](auto ct) { wda[ct] = L0 + (16 * kq + 4 * h + q4) * 128 + ((ct ^ (q4 >> 1)) * 64) + cs * 32 + p4 * 8; wdb[ct] = wda[ct] + 65536; PIN(wda[ct]); PIN(wdb[ct]); });
        wvo = (ln >> 3) * 1024 + (((ln & 7) ^ (cs << 2)) << 4); PIN(wvo);
        kv.ka = L0 + L_KV + sp * GQ * 1024 + ((ln & 51) | (ln & 4) << 1 | (ln & 8) >> 1) * 16; PIN(kv.ka);
        sfor<0, 3>([&](auto s) { va[s] = L0 + (r < 16 ? L_KV + 8192 + s * 16384 + sp * GQ * 1024 : L_CONST) + (h * 16 + (r & 15)) * 16; PIN(va[s]); });
        const unsigned hd = kq + 4 * (ln & 7);
        unsigned f0 = (rt >> 2) * 16 + ln, f1 = ((hd & 8) + c8) * 16 + 2 * (~hd & 4) + 4 * (hd >> 4) + (hd & 3); PIN(f0); PIN(f1);
        fo = WS_F2 + (ln < 16 ? f0 : ln < 24 ? f1 : (qt >> 2) * 16 + 6) * 8; PIN(fo);
    };
    {
        const bool light = cg < 8, four = (cg >> 1) == 3;
        const int u0 = light ? 5 * cg - (cg == 7) : 6 * cg - 10, nch = light ? (four ? 8 : 10) : 12;
        c.ws = ws; c.rsW = rsW; c.lane = lane; c.gst = cg == 6;
        c.rsB = __builtin_amdgcn_make_buffer_rsrc((void*)(p == 0 ? wg : wu), 0, DM * FF * 2, 0x20000);
        sfor<0, 3>([&](auto i) { const unsigned ch = 64 * i + lane - 12 * kr[i]; c.voB[i] = kr[i] * 2752 + (ch >= nch ? ch - 4 : ch) * 16; });
        c.soB = a * 16 * 2752 + u0 * 32;
        c.dB = L0 + p * UPOFF + a * 3072;
        { const unsigned one = ((tid & 11) == 0) ? 0x3F803F80u : 0u; lds128w(L0 + L_CONST + tid * 16, (u32x4){one, one, one, one}); }
        sfor<0, 4>([&](auto k) { c.xa[k] = L0 + (32 * a + r) * 128 + (((2 * k + h) ^ ((r >> 1) & 7)) << 4); });
        const unsigned wrow = L0 + (8 * h + q4) * 192 + p4 * 8;
        const int T0 = 3 * cg + 2 * p;
        c.sto = WS_Q + T0 * 131072 + (rb * 4 + a) * 1024;
        const float QS = 0.25f * LOG2E;
        c.qs0 = T0 < 16 ? QS : 1.f; c.qs1 = T0 + 1 < 16 ? QS : 1.f;
        c.wa = wrow + 16384 + cs * 32 + p * 128;
        const int ub = light ? 2 * p : 3 * p;
        c.ga = wrow + cs * UPOFF + ub * 32;
        c.f1 = ws + WS_F1 + (cg * 16 + rb) * 8;
        c.gwa = L0 + L_GW + h * 16; PIN(c.gwa);
        c.hso = WS_H + ((rb * 4 + a) * 86 + u0 + ub) * 1024;
        { const unsigned m = (lane >> 4) % 3; c.fp = ws + WS_F1 + ((m * 512 + head * 16) / 96 * 16 + (lane & 15)) * 8; }
        c.kzo = WS_K + head * 65536;
        c.kvo = c.kzo + (sp * (64 / NQ) + qg) * 1024;
        c.kzk = c.kzo + 32768 - (L0 + L_KV + sp * GQ * 1024);
        sfor<0, NQ>([&](auto i) { c.qo[i] = WS_Q + head * 65536 + (qb * 8 + qg * NQ + ((sp + 1 + i) & (NQ - 1))) * 1024; });
        c.oo = WS_O + ((qt * 4 + (head & 3)) * 8 + (head >> 2)) * 1024;
        c.kw = L0 + L_KV + w * 1024 + lane * 16;
        if (tid < 128) *(LAS unsigned*)(uintptr_t)(L0 + L_CNT + tid * 4) = 0;
        if (p == 0) { if (light) p1_loop<0, false, 2>(c, epoch, tag, kv, setup, pt1); else if (T0 >= 32) p1_loop<0, true, 0>(c, epoch, tag, kv, setup, pt1); else p1_loop<0, false, 0>(c, epoch, tag, kv, setup, pt1); }
        else { if (four) p1_loop<1, false, 1>(c, epoch, tag, kv, setup, pt1); else if (T0 >= 32) p1_loop<1, true, 0>(c, epoch, tag, kv, setup, pt1); else p1_loop<1, false, 0>(c, epoch, tag, kv, setup, pt1); }
    }
    asm volatile("s_waitcnt lgkmcnt(0)\n\ts_barrier" ::: "memory");
    if (p == 0) { kv.kf = lds128(kv.ka); kv.s = mfma(kv.kf, kv.q[0], kv.negm); }
    STAMP(5); STG(22);
    f32x16 accD[2]; accD[0] = f32x16{}; accD[1] = f32x16{};
    float gate = 0.f, gq = 0.f;
    u32x4 Hf[2][4], W5[4], H5[2], W3[8], H4[4], W2[8];
    unsigned ln16 = lane * 16;
    unsigned ob0 = WS_O + (rt * 4 + kq) * 8192, ob1 = ob0 + 4096; SPIN_(ob0); SPIN_(ob1);
    {
        const unsigned kvo = c.kvo;
        unsigned kw = c.kw; PIN(kw);
        u32x4 kst, vst, k2, v2;
        f32x16 negm = kv.negm;
        const rsrc_t rsD = __builtin_amdgcn_make_buffer_rsrc((void*)wd, 0, FF * DM * 2, 0x20000), rsO = __builtin_amdgcn_make_buffer_rsrc((void*)wo, 0, DM * DM * 2, 0x20000);
        const unsigned wso = w * 32768 + cb * 128, wdst = L0 + w * 4096;
        const rsrc_t rsX = __builtin_amdgcn_make_buffer_rsrc((void*)(w < 3 ? wd : wo), 0, w < 3 ? FF * DM * 2 : DM * DM * 2, 0x20000);
        const unsigned wsx = wso + (w < 3 ? 5 * 262144 : 5 * 32768), wsy = 13 * 32768 + w * 8192 + cb * 128, wdy = L0 + w * 1024;
        const unsigned ka = kv.ka;
        const unsigned hso = WS_H + (rt * 86 + kq) * 1024;
        constexpr int XO[3] = {L_KV + 32768, L_KV, L_KV - 16384};
        u32x4 Kf, V0, V1, Wf[2];
        unsigned sel = 0xDDDDDDDDu, one = 1, ca = L0 + L_CNT + lane * 4; PIN(sel); PIN(one); PIN(ca);
        f32x16 S[2], O[NQ]; u64 fv; unsigned arr;
        sfor<0, NQ>([&](auto i) { O[i] = f32x16{}; });
        Kf = kv.kf; V0 = lds128(va[0]);
        S[0] = kv.s;
        sfor<0, 64>([&](auto ii) {
            constexpr int i = ii, t = i / 8, k = i % 8, cu = i & 1, nx = cu ^ 1, cq = i % NQ, pq = (i + NQ - 1) % NQ;
            auto ex4 = [&](int o) { S[cu][o] = __builtin_amdgcn_exp2f(S[cu][o]); S[cu][o + 1] = __builtin_amdgcn_exp2f(S[cu][o + 1]); S[cu][o + 2] = __builtin_amdgcn_exp2f(S[cu][o + 2]); S[cu][o + 3] = __builtin_amdgcn_exp2f(S[cu][o + 3]); };
            auto ex = [&](int o) { S[cu][o] = __builtin_amdgcn_exp2f(S[cu][o]); };
            auto prio = [&](auto hh) {
                if constexpr (hh == 0) __builtin_amdgcn_s_setprio(3);
                if constexpr (hh == PRA) __builtin_amdgcn_s_setprio(2);
                if constexpr (hh == PRB) __builtin_amdgcn_s_setprio(1);
                if constexpr (hh == PRC) __builtin_amdgcn_s_setprio(0);
                SBAR();
            };
            constexpr auto dp = [](int s) { return s >= 24 && s < 64 - T4; };
            auto wdma = [&](auto uu) {
                constexpr int u = uu;
                if constexpr (t >= 1 && t < 6) dma<((t - 1) % 3) * 32768 + u * 1024>(rsD, wvo, wso + (t - 1) * 262144 + u * 8192, wdst);
                if constexpr (t == 6) {
                    dma<65536 + u * 1024>(rsX, wvo, wsx + u * 8192, wdst);
                }
                if constexpr (t == 7) {
                    dma<u * 1024>(rsO, wvo, wso + u * 8192, wdst);
                    if constexpr (u < 2) dma<98304 + u * 8192>(rsO, wvo, u && w < 4 ? wsy + 65536 : wsy, wdy);
                }
            };
            auto hld = [&](auto uu) {
                constexpr int u = uu;
                if constexpr (t >= 2 && t <= 6) (t == 6 ? H4[u] : Hf[(t + 1) & 1][u]) = ldc(rsW, lane * 16, hso + (16 * (t - 2) + 4 * u) * 1024);
                if constexpr (t == 7 && u == 0) H5[0] = ldc(rsW, lane * 16, hso + 80 * 1024);
                if constexpr (t == 7 && u == 1) H5[1] = ldc(rsW, lane * 16, hso + (kq < 2 ? 84 : 80) * 1024);
            };
            constexpr int LK = 2 * (cq == 0) + 2 * dp(i);
            auto mem = [&](auto mm) {
                constexpr int m = 3 * k + mm;
                constexpr int n = m - (t ? 3 * CK : 0), hm = t == 2 ? HP : HM;
                if constexpr (m == HP - 8 && t == 2) asm volatile("buffer_load_dwordx2 %0, %1, %2, 0 offen sc1" : "=v"(fv) : "v"(fo), "s"(rsW) : "memory");
                if constexpr (m == 0 && t == 0) k2 = ldc(rsW, lane * 16, kvo + 16384 / NQ);
                if constexpr (m == 3 && t == 0) v2 = ldc(rsW, lane * 16, kvo + 16384 / NQ + (2 << 20));
                if constexpr (m == 18 && t == 0) lds128w(kw + 32768, k2);
                if constexpr (m == 21 && t == 0) lds128w(kw + 32768 + 8192, v2);
                if constexpr (n == -2) asm volatile("ds_read_b32 %0, %1 offset:256" : "=v"(arr) : "v"(ca) : "memory");
                if constexpr (n == 0 && t > 0) {
                    asm volatile("s_waitcnt lgkmcnt(%3)\n\tv_cmp_gt_u32 vcc, %2, %0\n\ts_cbranch_vccz 2f\n"
                                 "1:\n\tds_read_b32 %0, %1 offset:256\n\ts_waitcnt lgkmcnt(0)\n\tv_cmp_gt_u32 vcc, %2, %0\n\ts_cbranch_vccnz 1b\n2:"
                                 : "+v"(arr) : "v"(ca), "n"(8 * t), "n"(LK) : "memory", "vcc");
                }
                if constexpr (n == 0) wdma(std::integral_constant<int, 0>{});
                if constexpr (n == 1) wdma(std::integral_constant<int, 1>{});
                if constexpr (n == 2) wdma(std::integral_constant<int, 2>{});
                if constexpr (n == 3) wdma(std::integral_constant<int, 3>{});
                if constexpr (n == 4 && t > 0 && t + 2 < 8) lds128w(kw + ((t + 2) % 3) * 16384, kst);
                if constexpr (n == 5 && t > 0 && t + 2 < 8) lds128w(kw + ((t + 2) % 3) * 16384 + 8192, vst);
                if constexpr (n == 6 && t + 3 < 8) kst = ldc(rsW, lane * 16, kvo + (t + 3) * (8192 / NQ));
                if constexpr (n == 7 && t + 3 < 8) vst = ldc(rsW, lane * 16, kvo + (t + 3) * (8192 / NQ) + (2 << 20));
                if constexpr (m == HP && t == 2) {
                    constexpr auto vm = [](int z) { z -= 3 * CK; return z >= 0 && z < 8 && z != 4 && z != 5; };
                    asm volatile("s_waitcnt vmcnt(%1)" : "+v"(fv) : "n"(vm(m - 8) + vm(m - 7) + vm(m - 6) + vm(m - 5) + vm(m - 4) + vm(m - 3) + vm(m - 2) + vm(m - 1) + vm(m)) : "memory");
                    if (!__all(fv == tag)) poll(ws + fo, tag);
                    gate = __builtin_bit_cast(float, __builtin_amdgcn_raw_buffer_load_b32(rsW, GF ? (lane >> 3) * 4 : r * 4, GF ? WS_G + (rt & ~1) * 128 + w * 32 : WS_G + rt * 128, 16));
                    gq = __builtin_bit_cast(float, __builtin_amdgcn_raw_buffer_load_b32(rsW, r * 4, WS_G + qt * 128, 16));
                }
                if constexpr (m >= hm && m < hm + 4) hld(std::integral_constant<int, m - hm>{});
                if constexpr (t == 0 && m == 3 * PUBH) {
                    asm volatile("s_waitcnt vmcnt(4)" ::: "memory");
                    STG(23);
                    arr = __hip_atomic_fetch_add((LAS unsigned*)(uintptr_t)ca, one, __ATOMIC_RELAXED, __HIP_MEMORY_SCOPE_WORKGROUP);
                }
                if constexpr (t == 0 && m == 3 * PUBH + 2) {
                    if (lane == 0 && arr == 7) __hip_atomic_store((GAS u64*)(ws + WS_F2 + (rb * 16 + cg) * 8), tag, RLX);
                }
                if constexpr (i > 64 - NQ && mm == 2) {
                    const int wd_ = ((sp + 1 + pq) & (NQ - 1)) * GQ + qg;
                    const unsigned xa = L0 + XO[pq] + wd_ * 2048 + lane * 16;
                    lds128w(xa, __builtin_bit_cast(u32x4, (f32x4){O[pq][0], O[pq][1], O[pq][2], O[pq][3]}));
                    lds128w(xa + 1024, __builtin_bit_cast(u32x4, (f32x4){O[pq][4], O[pq][5], O[pq][6], O[pq][7]}));
                    *(LAS float*)(uintptr_t)(L0 + L_XL + (pq * 8 + wd_) * 256 + lane * 4) = O[pq][8];
                }
                SBAR();
            };
            SBAR();
            if constexpr (k == 0 && t > 0) {
                asm volatile("s_waitcnt vmcnt(0)\n\tds_add_u32 %0, %1 offset:256" ::"v"(ca), "v"(one) : "memory");
                STG(23 + t);
                if constexpr (t + 2 < 8) { PIN(kst); PIN(vst); }
                if constexpr (t >= 3) sfor<0, 4>([&](auto u) { PIN(t == 7 ? H4[u] : Hf[t & 1][u]); });
                if constexpr (t == 3) { PIN(gate); PIN(gq); }
                SBAR();
            }
            prio(std::integral_constant<int, 2 * k>{});
            ex4(0); PIN(S[cu]);
            SBAR();
            if constexpr (i > 0) O[pq] = mfmah<8>(V1, S[nx], O[pq], sel);
            SBAR();
            if constexpr (cq == 0) V1 = lds128(va[t % 3] + (k / NQ) * 1024 + 512);
            mem(std::integral_constant<int, 0>{});
            ex4(4); PIN(S[cu]);
            SBAR();
            if constexpr (i + 1 < 64) S[nx] = mfma(Kf, kv.q[(i + 1) % NQ], NOM ? f32x16{} : negm);
            SBAR();
            if constexpr (i + 2 < 64 && (i + 2) % NQ == 0) Kf = lds128(ka + (((i + 2) / 8) % 3) * 16384 + ((i + 2) % 8 / NQ) * 1024);
            mem(std::integral_constant<int, 1>{});
            prio(std::integral_constant<int, 2 * k + 1>{});
            ex4(8); PIN(S[cu]);
            SBAR();
            O[cq] = mfmah<0>(V0, S[cu], O[cq], sel);
            SBAR();
            if constexpr (i + 1 < 64 && (i + 1) % NQ == 0) V0 = lds128(va[((i + 1) / 8) % 3] + ((i + 1) % 8 / NQ) * 1024);
            mem(std::integral_constant<int, 2>{});
            ex4(12); PIN(S[cu]);
            SBAR();
            if constexpr (dp(i) && i < 56 - T3 - T2) accD[k & 1] = mfma(Wf[cu], Hf[t & 1][k >> 1], accD[k & 1]);
            SBAR();
            if constexpr (dp(i + 1)) {
                constexpr int t1 = (i + 1) / 8, k1 = (i + 1) % 8, wslot = (t1 % 3) * 32768;
                (i + 1 < 56 - T3 - T2 ? Wf[nx] : i + 1 < 48 ? W2[k1] : W3[k1]) = ldstr2((wslot == 65536 ? wdb[k1 & 1] : wda[k1 & 1] + wslot) + (k1 >> 1) * 8192, 1024);
            }
        });
        SBAR();
        f32x16 R = mfmah<8>(V1, S[1], O[NQ - 1], sel);
        STAMP(6); STG(31);
        waitbar<0>();
        PIN(H5[0]); PIN(H5[1]);
        sfor<0, NQ - 1>([&](auto e) {
            const unsigned xa = L0 + XO[e] + w * 2048 + lane * 16;
            const f32x4 lo = __builtin_bit_cast(f32x4, lds128(xa)), hi = __builtin_bit_cast(f32x4, lds128(xa + 1024));
            const float xl = *(const LAS float*)(uintptr_t)(L0 + L_XL + (e * 8 + w) * 256 + lane * 4);
            SBAR();
            sfor<0, 4>([&](auto z) { W5[z] = ldstr2(wdb[z & 1] + (z >> 1) * 8192, 1024); });
            SBAR();
            sfor<0, 4>([&](auto z) { R[+z] += lo[+z]; R[4 + z] += hi[+z]; });
            R[8] += xl;
        });
        const f32x2 sm = ((f32x2){R[0], R[1]} + (f32x2){R[2], R[3]}) + ((f32x2){R[4], R[5]} + (f32x2){R[6], R[7]});
        const bool bad = !(__builtin_amdgcn_fmed3f(R[8], 0x1p-80f, 0x1p100f) + (sm[0] + sm[1]) * 0.f == R[8]);
        if (__builtin_expect(__any(bad), 0)) {
            const unsigned hk = c.kzo; const u32x4 Q = kv.q[NQ - 1];
            float m = -INFINITY;
#pragma unroll 1
            for (int i = 0; i < 64; ++i) {
                const f32x16 s = mfma(ldc(rsW, lane * 16, hk + i * 1024), Q, f32x16{});
                sfor<0, 16>([&](auto e) { m = fmaxf(m, s[+e]); });
            }
            auto rr = __builtin_amdgcn_permlane32_swap(__float_as_uint(m), __float_as_uint(m), false, false);
            m = -fmaxf(__uint_as_float(rr[0]), __uint_as_float(rr[1]));
            sfor<0, 16>([&](auto i) { negm[+i] = m; });
            R = f32x16{};
            const unsigned one = ((r & 11) == 0) ? 0x3F803F80u : 0u;
            const u32x4 cv = {one, one, one, one};
#pragma unroll 1
            for (int i = 0; i < 64; ++i) {
                f32x16 s = mfma(ldc(rsW, lane * 16, hk + i * 1024), Q, negm);
                u32x4 va0 = ldc(rsW, (h * 16 + (r & 15)) * 16, hk + (2 << 20) + i * 1024), va1 = ldc(rsW, (h * 16 + (r & 15)) * 16 + 512, hk + (2 << 20) + i * 1024);
                if (r >= 16) { va0 = cv; va1 = cv; }
                sfor<0, 16>([&](auto e) { s[+e] = __builtin_amdgcn_exp2f(s[+e]); });
                R = mfma(va0, pack8(s, 0, 1.f), R);
                R = mfma(va1, pack8(s, 8, 1.f), R);
            }
        }
        u32x4 ov = pack8(R, 0, gq * __builtin_amdgcn_rcpf(R[8]));
        sfor<0, 4>([&](auto e) { ov[+e] = min(ov[+e], 0xFFFFFFFEu); });
        stc(ov, rsW, lane * 16, c.oo);
    }
    STAMP(7); STG(32);
    {
        SBAR();
        u32x4 W4[8];
        {
            sfor<8 - T4, 8>([&](auto k) { W4[k] = ldstr2(wda[k & 1] + 32768 + (k >> 1) * 8192, 1024); });
            SBAR();
            static_assert(T2 == 0 || T3 == 8, "");
            sfor<8 - T2, 8>([&](auto k) { accD[k & 1] = mfma(W2[k], Hf[1][k >> 1], accD[k & 1]); });
            sfor<8 - T3, 8 - T3 + E3>([&](auto k) { accD[k & 1] = mfma(W3[k], Hf[0][k >> 1], accD[k & 1]); });
            SBAR();
            if (S5E) { const unsigned mk = kq < 2 ? ~0u : 0u; H5[1] &= mk; sfor<0, 4>([&](auto e) { accD[e & 1] = mfma(W5[e], H5[e >> 1], accD[e & 1]); }); }
            SBAR();
            sfor<0, E4>([&](auto k) { accD[k & 1] = mfma(W4[k], H4[k >> 1], accD[k & 1]); });
            SBAR();
            sfor<E4, 8>([&](auto k) { PIN(W4[k]); });
        }
        if (!S5E) sfor<0, 4>([&](auto z) { PIN(W5[z]); });
        u32x32 Of, Wa, Wb;
#define P3IN "v"(ln16), "s"(rsW), "s"(ob0), "s"(ob1), "v"(wda[0]), "v"(wda[1]), "v"(wdb[0]), "v"(wdb[1])
#define P3MAC(a, b, c, d, e, f, g, h) \
        ".macro OLD v\n .if \\v < 4\n buffer_load_dwordx4 v[128+4*\\v:131+4*\\v], " a ", " b ", " c " offen offset:1024*\\v sc1\n .else\n" \
        " buffer_load_dwordx4 v[128+4*\\v:131+4*\\v], " a ", " b ", " d " offen offset:1024*\\v-4096 sc1\n .endif\n.endm\n" \
        ".macro WRD v, p, q, o\n ds_read_b64_tr_b16 v[160+8*\\v:161+8*\\v], \\p offset:\\o\n ds_read_b64_tr_b16 v[162+8*\\v:163+8*\\v], \\p offset:\\o+1024\n" \
        " ds_read_b64_tr_b16 v[164+8*\\v:165+8*\\v], \\q offset:\\o\n ds_read_b64_tr_b16 v[166+8*\\v:167+8*\\v], \\q offset:\\o+1024\n.endm\n" \
        ".macro REQ v\n .if \\v < 4 || " STR(EO) " == 0\n OLD \\v\n .endif\n .if \\v < 4\n WRD \\v, " e ", " f ", 8192*\\v\n .else\n WRD \\v, " g ", " h ", 8192*\\v-20480\n .endif\n.endm\n"
#define P3END ".purgem OLD\n.purgem WRD\n.purgem REQ\n"
        static_assert(LD == 4 && !EO, "");
        asm volatile(P3MAC("%2", "%3", "%4", "%5", "%6", "%7", "%8", "%9")
            "s_barrier\n .irp v, 0, 1, 2, 3, 4, 5, 6, 7\n .if \\v < " STR(LD) "\n REQ \\v\n .endif\n .endr\n" P3END
            : "=&{v[128:159]}"(Of), "=&{v[160:191]}"(Wa) : P3IN : "memory");
        SBAR();
#define DSTAMP(o)
        u64 dta, dtl;
        sfor<8 - T3 + E3, 8>([&](auto k) { accD[k & 1] = mfma(W3[k], Hf[0][k >> 1], accD[k & 1]); });
        sfor<E4, 9>([&](auto k) {
            if constexpr (EO && k == X4) { SBAR(); asm volatile(P3MAC("%1", "%2", "%3", "%4", "%5", "%6", "%7", "%8") " OLD 4\n OLD 5\n OLD 6\n OLD 7\n" P3END : "+{v[128:159]}"(Of) : P3IN : "memory"); SBAR(); }
            if constexpr (k < 8) accD[k & 1] = mfma(W4[k], H4[k >> 1], accD[k & 1]);
        });
        if (!S5E) { const unsigned mk = kq < 2 ? ~0u : 0u; H5[1] &= mk; sfor<0, 4>([&](auto e) { accD[e & 1] = mfma(W5[e], H5[e >> 1], accD[e & 1]); }); }
        auto rbase = [](unsigned i) { return i * 8704 + (i < 3 ? 32768 : 110592 - 3 * 8704); };
        unsigned ra = L0 + rbase(w) + r * 272 + h * 16, rr[4];
        sfor<0, 4>([&](auto k) { rr[k] = L0 + rbase(2 * k + (w >> 2)) + ((tid >> 3) & 31) * 272 + (tid & 7) * 32; PIN(rr[k]); });
        unsigned oa = (256 * c8 + 64 * (j >> 3) + (tid >> 3)) * 1024 + cb * 128 + (tid & 7) * 16; PIN(ra); PIN(oa);
        rsrc_t rsY = __builtin_amdgcn_make_buffer_rsrc(out, 0, SEQ * DM * 2, 0x20000); SPIN_(rsY);
        float g1 = GF ? __builtin_amdgcn_rcpf(1.f + gate) : 1.f - gate; PIN(g1);
        SBAR();
        if (!GF) sfor<0, 2>([&](auto ct) { accD[ct] *= g1; PIN(accD[ct]); });
        SBAR();
        unsigned it = RM, tv, ts;
        asm volatile(P3MAC("%10", "%11", "%12", "%13", "%14", "%15", "%16", "%17")
            ".macro CHK v, l\n v_max3_u32 %6, v[128+4*\\v], v[129+4*\\v], v[130+4*\\v]\n v_max_u32 %6, %6, v[131+4*\\v]\n v_cmp_eq_u32 vcc, -1, %6\n s_cbranch_vccnz \\l\\v\n.endm\n"
            ".macro USE v\n v_mfma_f32_32x32x16_bf16 %3, v[160+8*\\v:163+8*\\v], v[128+4*\\v:131+4*\\v], %3\n v_mfma_f32_32x32x16_bf16 %4, v[164+8*\\v:167+8*\\v], v[128+4*\\v:131+4*\\v], %4\n.endm\n"
            ".irp v, 0, 1, 2, 3, 4, 5, 6, 7\n"
            " .if \\v + " STR(LD) " < 8\n REQ (\\v+" STR(LD) ")\n .set .Lq, " STR(LD) "\n .else\n .set .Lq, 7-\\v\n .endif\n"
            " .if " STR(EO) "\n .set .Lv, 7-\\v\n .else\n .set .Lv, .Lq\n .endif\n"
            " .if .Lq > 3\n s_waitcnt vmcnt(.Lv) lgkmcnt(14)\n .else\n s_waitcnt vmcnt(.Lv) lgkmcnt(4*.Lq)\n .endif\n"
            " CHK \\v, .LX\n USE \\v\n"
            ".endr\n s_branch .LE\n"
            ".irp v, 0, 1, 2, 3, 4, 5, 6, 7\n"
            ".LX\\v:\n OLD \\v\n"
            " .if \\v < 3\n s_waitcnt vmcnt(\\v+2)\n .irp u, 1, 2, 3\n .if \\u > \\v\n v_cmpx_eq_u32 vcc, -1, v[131+4*\\u]\n OLD \\u\n s_mov_b64 exec, -1\n .endif\n .endr\n .endif\n"
            " .irp u, 1, 2, 3, 4, 5, 6, 7\n .if \\u > \\v + 4\n REQ \\u\n .endif\n .endr\n" DSTAMP("%8") " s_waitcnt vmcnt(0) lgkmcnt(0)\n s_branch .LS\\v\n"
            ".LR\\v:\n s_add_u32 %5, %5, 1\n" DSTAMP("%9")
            " .if \\v < 7\n"
            " .irp u, 1, 2, 3, 4, 5, 6, 7\n .if \\u > \\v\n v_cmpx_eq_u32 vcc, -1, v[131+4*\\u]\n OLD \\u\n s_mov_b64 exec, -1\n .endif\n .endr\n"
            " .endif\n"
            ".LP\\v:\n OLD \\v\n s_cmp_lt_u32 %5, 0x40000\n"
            " s_waitcnt vmcnt(0)\n"
            " s_cbranch_scc1 .LS\\v\n s_branch .LM\\v\n"
            ".endr\n"
            ".irp v, 0, 1, 2, 3, 4, 5, 6, 7\n.LS\\v:\n CHK \\v, .LR\n.LM\\v:\n USE \\v\n.endr\n"
            ".LE:\n s_nop 11\n" P3END ".purgem CHK\n.purgem USE\n"
            : "+{v[128:159]}"(Of), "+{v[160:191]}"(Wa), "=&{v[192:223]}"(Wb), "+v"(accD[0]), "+v"(accD[1]), "+s"(it), "=&v"(tv), "=&s"(ts), "+s"(dta), "+s"(dtl) : P3IN : "memory", "vcc", "scc");
        STAMP(8); STG(33);
        sfor<0, 2>([&](auto ct) { sfor<0, 4>([&](auto g) {
            lds128w(ra + ct * 128 + g * 32, __builtin_bit_cast(u32x4, (f32x4){accD[ct][4 * g], accD[ct][4 * g + 1], accD[ct][4 * g + 2], accD[ct][4 * g + 3]}));
        }); });
        asm volatile("s_waitcnt lgkmcnt(0)\n\ts_barrier" ::: "memory");
        f32x4 pl[4], ph[4];
        sfor<0, 4>([&](auto k) { pl[k] = __builtin_bit_cast(f32x4, lds128(rr[k])); ph[k] = __builtin_bit_cast(f32x4, lds128(rr[k] + 16)); });
        SBAR();
        const f32x4 lo = ((pl[0] + pl[1]) + (pl[2] + pl[3])) * (GF ? g1 : 1.f), hi = ((ph[0] + ph[1]) + (ph[2] + ph[3])) * (GF ? g1 : 1.f);
        __builtin_amdgcn_raw_buffer_store_b128((u32x4){pk(lo[0], lo[1]), pk(lo[2], lo[3]), pk(hi[0], hi[1]), pk(hi[2], hi[3])}, rsY, oa, 0, 16);
        asm volatile("s_waitcnt vmcnt(1)" : "+{v[128:159]}"(Of)::"memory");
        asm volatile(".Lk_end:");
        if (b == 0 && tid == 0) __hip_atomic_store((GAS u64*)ws, epoch + 1, RLX);
    }
    STAMP(9); STG(34);
}

extern "C" void kernel_launch(void* const* d_in, const int* in_sizes, int n_in, void* d_out, int out_size, void* d_ws, size_t ws_size, hipStream_t stream) {
    (void)in_sizes; (void)n_in; (void)out_size; (void)ws_size;
    (void)hipFuncSetAttribute((const void*)fused_kernel, hipFuncAttributeMaxDynamicSharedMemorySize, LDS_BYTES);
    hipLaunchKernelGGL(fused_kernel, dim3(256), dim3(512), LDS_BYTES, stream, (const char*)d_in[0], (const char*)d_in[1], (const char*)d_in[2], (const char*)d_in[4],
                       (const char*)d_in[5], (const char*)d_in[6], (const char*)d_in[7], (char*)d_out, (char*)d_ws);
}
```
